```python
import torch

def _softmax(x, axis=-1):
    return torch.softmax(x, dim=axis)
def _gelu(x):
    return torch.nn.functional.gelu(x, approximate="tanh")
def _silu(x):
    return torch.nn.functional.silu(x)
def _sigmoid(x):
    return torch.sigmoid(x)
def _layernorm(x, axis=-1, eps=1e-5):
    m = x.mean(dim=axis, keepdim=True)
    v = x.var(dim=axis, keepdim=True, unbiased=False)
    return (x - m) / torch.sqrt(v + eps)
def _scatter_add(tgt, idx, src, axis=0):
    return tgt.clone().index_add_(axis, idx, src)
def _segment_sum(src, seg, n):
    out = torch.zeros((n,) + src.shape[1:], dtype=src.dtype)
    return out.index_add_(0, seg, src)
def _pad(x, pw):
    flat = tuple(v for pair in reversed(pw) for v in pair)
    return torch.nn.functional.pad(x, flat)

kernel_name = 'gated_mix__grouped_kv_mqa__compressed_kv_cla__prompt'

def setup_inputs():

    _w = (lambda a: (lambda t: t.to(torch.bfloat16) if t.is_floating_point() else t)(torch.as_tensor(a)))
    def _A_setup():
        B, S, D, H = 1, 2048, 512, 32
        Dh = D // H
        return {
            "x": _w((lambda *s: torch.randn(*s, dtype=torch.bfloat16) * 0.05)(B, S, D)),
            "wq": _w((lambda *s: torch.randn(*s, dtype=torch.bfloat16) * 1.6)(D, H * Dh)),
            "wk": _w((lambda *s: torch.randn(*s, dtype=torch.bfloat16) * 1.6)(D, Dh)),
            "wv": _w((lambda *s: torch.randn(*s, dtype=torch.bfloat16) * 0.8)(D, Dh)),
            "wo": _w((lambda *s: torch.randn(*s, dtype=torch.bfloat16) * 0.8)(H * Dh, D)),
        }
    _A_in = _A_setup()

    def _B_setup():
        B, H, Sq, Sk, D, M = 1, 32, 2048, 2048, 128, 512
        Dc = 4 * D
        return {
            "x": _w((lambda *s: torch.randn(*s, dtype=torch.bfloat16) * 0.05)(B, Sq, M)),
            "c_kv": _w((lambda *s: torch.randn(*s, dtype=torch.bfloat16) * 1.6)(B, Sk, Dc)),
            "w_q": _w((lambda *s: torch.randn(*s, dtype=torch.bfloat16) * 6.4)(M, H, D) * (M ** -0.5)),
            "w_uk": _w((lambda *s: torch.randn(*s, dtype=torch.bfloat16) * 6.4)(Dc, H, D) * (Dc ** -0.5)),
            "w_uv": _w((lambda *s: torch.randn(*s, dtype=torch.bfloat16) * 1.6)(Dc, H, D) * (Dc ** -0.5)),
            "w_o": _w((lambda *s: torch.randn(*s, dtype=torch.bfloat16) * 1.6)(H, D, M) * ((H * D) ** -0.5)),
        }
    _B_in = _B_setup()

    return {
        "A_x": _A_in["x"],
        "A_wq": _A_in["wq"],
        "A_wk": _A_in["wk"],
        "A_wv": _A_in["wv"],
        "A_wo": _A_in["wo"],
        "B_x": _B_in["x"],
        "B_c_kv": _B_in["c_kv"],
        "B_w_q": _B_in["w_q"],
        "B_w_uk": _B_in["w_uk"],
        "B_w_uv": _B_in["w_uv"],
        "B_w_o": _B_in["w_o"],
        "gate_w": _w((lambda *s: torch.randn(*s, dtype=torch.bfloat16) * 0.1)(512) * 16.0),
    }

def reference(A_x, A_wq, A_wk, A_wv, A_wo, B_x, B_c_kv, B_w_q, B_w_uk, B_w_uv, B_w_o, gate_w):
    def _A_ref(x, wq, wk, wv, wo):
        B, S, D = x.shape
        H = 32
        Dh = D // H
        q = (lambda x, *d: x.permute(*d))((x @ wq).reshape(B, S, H, Dh), 0, 2, 1, 3)
        k = x @ wk
        v = x @ wv
        scores = torch.einsum("bhqd,bkd->bhqk", q, k) * (Dh ** -0.5)
        p = _softmax(scores, axis=-1)
        o = torch.einsum("bhqk,bkd->bhqd", p, v)
        return (lambda x, *d: x.permute(*d))(o, 0, 2, 1, 3).reshape(B, S, H * Dh) @ wo

    def _B_ref(x, c_kv, w_q, w_uk, w_uv, w_o):
        B, Sq, M = x.shape[0], x.shape[1], x.shape[2]
        Sk = c_kv.shape[1]
        H, D = w_q.shape[1], w_q.shape[2]
        q = torch.einsum("bqm,mhd->bhqd", x, w_q)
        k = torch.einsum("bkc,chd->bhkd", c_kv, w_uk)
        v = torch.einsum("bkc,chd->bhkd", c_kv, w_uv)
        scores = torch.einsum("bhqd,bhkd->bhqk", q, k) * (D ** -0.5)
        off = Sk - Sq
        qi = torch.arange(Sq)[:, None]
        kj = torch.arange(Sk)[None, :]
        cmask = kj <= (qi + off)
        scores = torch.where(cmask[None, None], scores, -1e9)
        p = _softmax(scores, axis=-1)
        ctx = torch.einsum("bhqk,bhkd->bhqd", p, v)
        return torch.einsum("bhqd,hdm->bqm", ctx, w_o)

    x = A_x
    g = _sigmoid((x * gate_w).sum(-1, keepdims=True))
    a = _A_ref(x, A_wq, A_wk, A_wv, A_wo)
    b = _B_ref(x, B_c_kv, B_w_q, B_w_uk, B_w_uv, B_w_o)
    return g * a + (1 - g) * b
```

```cpp
#include <hip/hip_runtime.h>
#include <hip/hip_bf16.h>
#include <stdint.h>

typedef short bf16x8 __attribute__((ext_vector_type(8)));
typedef short s16x4 __attribute__((ext_vector_type(4)));
typedef float f32x16 __attribute__((ext_vector_type(16)));
typedef float f32x4 __attribute__((ext_vector_type(4)));
typedef float f32x2 __attribute__((ext_vector_type(2)));
typedef __bf16 bf16x2_t __attribute__((ext_vector_type(2)));
typedef unsigned u32x4 __attribute__((ext_vector_type(4)));
typedef __attribute__((address_space(3))) const char* lds_cptr;
typedef __attribute__((address_space(3))) char* lds_ptr;

#define DI __device__ __forceinline__
#define MFMA(a, b, c) __builtin_amdgcn_mfma_f32_32x32x16_bf16((a), (b), (c), 0, 0, 0)
#define MFMA16(a, b, c) __builtin_amdgcn_mfma_f32_16x16x32_bf16((a), (b), (c), 0, 0, 0)
#define SBAR() __builtin_amdgcn_sched_barrier(0)
#define WAIT_BAR(N) asm volatile("s_waitcnt vmcnt(" #N ") lgkmcnt(0)\n\ts_barrier" ::: "memory")
#define LGKM_BAR() asm volatile("s_waitcnt lgkmcnt(0)\n\ts_barrier" ::: "memory")

constexpr int SEQ = 2048, DM = 512, NH = 32;
constexpr float LOG2E = 1.4426950408889634f;
constexpr float QSCALE_B = LOG2E * 0.08838834764831845f;
constexpr float QSCALE_A = LOG2E * 0.25f;
#define L_LIMIT_V 1.099511627776e12f
constexpr float L_LIMIT = L_LIMIT_V;

#define WARM2 1
#define WARM_KT 9
#define WARM3 0
#define QP_APRIO 1
#define B_PRIO 1
#define A_PRIO 0
#define APRIO_HI() do { } while (0)
#define APRIO_LO() do { } while (0)
constexpr size_t WS_FLAG = 0;
constexpr int FL_KV = 0;
constexpr int FL_P1 = 1024;
constexpr int FL_EARLY = 1280;
constexpr int FL_LATE = FL_EARLY + 1024;
constexpr size_t WS_FLAG_BYTES = 32768;
constexpr size_t WS_KVB = WS_FLAG_BYTES;
constexpr size_t WS_KVA = WS_KVB + (size_t)32 * 32 * 32768;
constexpr size_t WS_GATE = WS_KVA + (size_t)32 * 6144;
constexpr size_t WS_CTX = WS_GATE + 8192;
constexpr unsigned SPIN_CAP = 1u << 18;

#define STAMP(ws_, idx) do { } while (0)
#define TL(var, idx) do { } while (0)
#define TLOUT(var, slot) do { } while (0)
DI int lane_id() { int l; asm volatile("v_mbcnt_lo_u32_b32 %0, -1, 0\n\tv_mbcnt_hi_u32_b32 %0, -1, %0" : "=v"(l)); return l; }
DI int rfl(int x) { return __builtin_amdgcn_readfirstlane(x); }
DI const char* uptr(const char* p) { const unsigned long long v = (unsigned long long)p;
    return (const char*)(((unsigned long long)(unsigned)rfl((int)(v >> 32)) << 32) | (unsigned)rfl((int)v)); }
DI unsigned cvtpk(float lo, float hi) { f32x2 v = {lo, hi}; bf16x2_t b = __builtin_convertvector(v, bf16x2_t); return __builtin_bit_cast(unsigned, b); }
template <int S> DI bf16x8 pack8(const f32x16& a) {
    u32x4 w = {cvtpk(a[8 * S], a[8 * S + 1]), cvtpk(a[8 * S + 2], a[8 * S + 3]), cvtpk(a[8 * S + 4], a[8 * S + 5]), cvtpk(a[8 * S + 6], a[8 * S + 7])};
    return __builtin_bit_cast(bf16x8, w);
}
template <int S> DI bf16x8 pack8s(const f32x16& a, float c) {
    u32x4 w = {cvtpk(a[8 * S] * c, a[8 * S + 1] * c), cvtpk(a[8 * S + 2] * c, a[8 * S + 3] * c), cvtpk(a[8 * S + 4] * c, a[8 * S + 5] * c), cvtpk(a[8 * S + 6] * c, a[8 * S + 7] * c)};
    return __builtin_bit_cast(bf16x8, w);
}
DI bf16x8 pack44(const f32x4& a, const f32x4& b) { u32x4 w = {cvtpk(a[0], a[1]), cvtpk(a[2], a[3]), cvtpk(b[0], b[1]), cvtpk(b[2], b[3])}; return __builtin_bit_cast(bf16x8, w); }
DI bf16x8 pack44s(const f32x4& a, const f32x4& b, float c) {
    u32x4 w = {cvtpk(a[0] * c, a[1] * c), cvtpk(a[2] * c, a[3] * c), cvtpk(b[0] * c, b[1] * c), cvtpk(b[2] * c, b[3] * c)}; return __builtin_bit_cast(bf16x8, w); }
DI bf16x8 lds_b128(lds_cptr p) { return *(const __attribute__((address_space(3))) bf16x8*)p; }
DI f32x4 lds_f4(lds_cptr p) { return *(const __attribute__((address_space(3))) f32x4*)p; }
DI void lds_st_b128(lds_ptr p, bf16x8 v) { *(__attribute__((address_space(3))) bf16x8*)p = v; }
DI void lds_st_f4(lds_ptr p, f32x4 v) { *(__attribute__((address_space(3))) f32x4*)p = v; }
DI s16x4 lds_tr(lds_cptr p) { return __builtin_amdgcn_ds_read_tr16_b64_v4i16((__attribute__((address_space(3))) s16x4*)p); }
DI bf16x8 lds_tr2(lds_cptr p0, lds_cptr p1) { s16x4 a = lds_tr(p0), b = lds_tr(p1); return __builtin_shufflevector(a, b, 0, 1, 2, 3, 4, 5, 6, 7); }
DI void glds1(const void* sbase, unsigned voff, unsigned lds_addr) {
    asm volatile("s_mov_b32 m0, %2\n\ts_nop 0\n\tglobal_load_lds_dwordx4 %0, %1" ::"v"(voff), "s"(sbase), "s"(lds_addr) : "memory");
}
DI void glds2(const void* sbase, unsigned voff, unsigned lds_addr) {
    asm volatile("s_mov_b32 m0, %2\n\ts_nop 0\n\tglobal_load_lds_dwordx4 %0, %1\n\tglobal_load_lds_dwordx4 %0, %1 offset:1024" ::"v"(voff), "s"(sbase), "s"(lds_addr) : "memory");
}
DI void glds4(const void* sbase, unsigned voff, unsigned lds_addr) {
    asm volatile("s_mov_b32 m0, %2\n\ts_nop 0\n\tglobal_load_lds_dwordx4 %0, %1\n\tglobal_load_lds_dwordx4 %0, %1 offset:1024\n\t"
                 "global_load_lds_dwordx4 %0, %1 offset:2048\n\tglobal_load_lds_dwordx4 %0, %1 offset:3072" ::"v"(voff), "s"(sbase), "s"(lds_addr) : "memory");
}
template <int OFF> DI void gstore(const void* sbase, unsigned voff, bf16x8 v) {
    asm volatile("global_store_dwordx4 %0, %1, %2 offset:%3 sc1\n\ts_nop 1" ::"v"(voff), "v"(v), "s"(sbase), "i"(OFF) : "memory");
}
DI void touch(const void* sbase, unsigned voff, unsigned lds_scratch) {
    asm volatile("s_mov_b32 m0, %2\n\ts_nop 4\n\tglobal_load_lds_dword %0, %1" ::"v"(voff), "s"(sbase), "s"(lds_scratch) : "memory");
}
typedef __attribute__((address_space(1))) unsigned gu32;
extern "C" __device__ unsigned long long amd_dispatch_id() __asm("llvm.amdgcn.dispatch.id");
DI unsigned launch_token() {
    const unsigned long long d = amd_dispatch_id(), q = (unsigned long long)__builtin_amdgcn_dispatch_ptr() - (d << 6);
    return ((unsigned)d + (unsigned)(d >> 32)) * 0x9E3779B1u + (unsigned)(q >> 24) * 0x85EBCA6Bu + 0x7F4A7C15u;
}
DI void flag_set(char* ws, int word, unsigned tok) { __hip_atomic_store((gu32*)(ws + WS_FLAG) + word, tok, __ATOMIC_RELAXED, __HIP_MEMORY_SCOPE_AGENT); }
template <int N, int MASK> DI void flag_wait(char* ws, int word, unsigned tok) {
    for (unsigned spins = 0; spins < SPIN_CAP; ++spins) {
        bool ok = true;
#pragma unroll
        for (int u = 0; u < N; ++u) ok = ok && __hip_atomic_load((gu32*)(ws + WS_FLAG) + word + 64 * u + (lane_id() & MASK), __ATOMIC_RELAXED, __HIP_MEMORY_SCOPE_AGENT) == tok;
        if (__all(ok)) break;
        __builtin_amdgcn_s_sleep(2);
    }
    asm volatile("" ::: "memory");
}
DI void glds1s(const void* sbase, unsigned voff, unsigned lds_addr) {
    asm volatile("s_mov_b32 m0, %2\n\ts_nop 0\n\tglobal_load_lds_dwordx4 %0, %1 sc1" ::"v"(voff), "s"(sbase), "s"(lds_addr) : "memory");
}
DI float swap_half(float x) {
    auto r = __builtin_amdgcn_permlane32_swap(__float_as_uint(x), __float_as_uint(x), false, false);
    return __uint_as_float((lane_id() & 32) ? r[0] : r[1]);
}
DI float grp_max(float x) {
    auto r = __builtin_amdgcn_permlane16_swap(__float_as_uint(x), __float_as_uint(x), false, false); x = fmaxf(__uint_as_float(r[0]), __uint_as_float(r[1]));
    auto u = __builtin_amdgcn_permlane32_swap(__float_as_uint(x), __float_as_uint(x), false, false); return fmaxf(__uint_as_float(u[0]), __uint_as_float(u[1]));
}
DI float grp_sum(float x) {
    auto r = __builtin_amdgcn_permlane16_swap(__float_as_uint(x), __float_as_uint(x), false, false); x = __uint_as_float(r[0]) + __uint_as_float(r[1]);
    auto u = __builtin_amdgcn_permlane32_swap(__float_as_uint(x), __float_as_uint(x), false, false); return __uint_as_float(u[0]) + __uint_as_float(u[1]);
}
DI float max16(const f32x4& a, const f32x4& b, const f32x4& c, const f32x4& d) {
    return fmaxf(fmaxf(fmaxf(fmaxf(a[0], a[1]), fmaxf(a[2], a[3])), fmaxf(fmaxf(b[0], b[1]), fmaxf(b[2], b[3]))),
                 fmaxf(fmaxf(fmaxf(c[0], c[1]), fmaxf(c[2], c[3])), fmaxf(fmaxf(d[0], d[1]), fmaxf(d[2], d[3]))));
}
DI float dot8(bf16x8 a, bf16x8 b, float acc) {
    const u32x4 ua = __builtin_bit_cast(u32x4, a), ub = __builtin_bit_cast(u32x4, b);
#pragma unroll
    for (int e = 0; e < 4; ++e) { const unsigned ta = ua[e], tb = ub[e]; acc = __builtin_amdgcn_fdot2_f32_bf16(__builtin_bit_cast(bf16x2_t, ta), __builtin_bit_cast(bf16x2_t, tb), acc, false); }
    return acc;
}
template <int CTRL, int RM> DI float dpp_add(float v) { return v + __int_as_float(__builtin_amdgcn_update_dpp(0, __float_as_int(v), CTRL, RM, 0xf, false)); }
DI float wave_sum63(float v) {
    v = dpp_add<0x111, 0xf>(v); v = dpp_add<0x112, 0xf>(v); v = dpp_add<0x114, 0xf>(v); v = dpp_add<0x118, 0xf>(v);
    v = dpp_add<0x142, 0xa>(v); v = dpp_add<0x143, 0xc>(v);
    return v;
}
DI float bf2f(short v) { return __uint_as_float(((unsigned)(unsigned short)v) << 16); }
DI float ex2(float x) { return __builtin_amdgcn_exp2f(x); }
DI float rowmax32(const f32x16& a, const f32x16& b) {
    float m0 = fmaxf(a[0], b[0]), m1 = fmaxf(a[1], b[1]);
#pragma unroll
    for (int r = 2; r < 16; r += 2) { m0 = fmaxf(m0, fmaxf(a[r], b[r])); m1 = fmaxf(m1, fmaxf(a[r + 1], b[r + 1])); }
    float m = fmaxf(m0, m1);
    return fmaxf(m, swap_half(m));
}
DI bool finite_f(float x) { return fabsf(x) <= 3.4028234e38f; }

constexpr int PSTG = 53248;
constexpr int KS0 = 106496, KS1 = 122880, KS2 = 16384;
constexpr int VS0 = 139264, VS1 = 0, VS2 = 32768, VS3 = 53248;
constexpr int PARK = 69632;
constexpr int CNT = 159744;
constexpr int GTAB = 160768;
constexpr int SCR = 162816;
#define PIN(x) asm volatile("" : "+v"(x))
DI void setm0(unsigned v) { asm volatile("s_mov_b32 m0, %0\n\ts_nop 0" ::"s"(v) : "memory"); }
template <int OFF> DI void gl(const void* sbase, unsigned voff) { asm volatile("global_load_lds_dwordx4 %0, %1 offset:%2 sc1" ::"v"(voff), "s"(sbase), "i"(OFF) : "memory"); }

struct Args {
    const char *x, *wqA, *wk, *wv, *woA, *ckv, *wqB, *wuk, *wuv, *woB, *gate_w;
    char *ws, *out;
};
typedef const __attribute__((address_space(4))) Args* ArgP;
DI ArgP args() { ArgP p = (ArgP)__builtin_amdgcn_kernarg_segment_ptr(); asm volatile("" : "+s"(p)); return p; }

template <int ROLE> DI void qp_piece(int kt, int p, const char* xsrc, const char* wqsrc, const char* wasrc, unsigned lds0, int w, unsigned x_voff, unsigned w_voff, unsigned a_voff) {
    const int pb = (kt % 3) * PSTG;
    if (p < 4) glds1(xsrc + p * 8192 + kt * 128, (p & 1) ? (x_voff ^ 64) : x_voff, lds0 + pb + w * 4096 + p * 1024);
    else if (p < 6) glds1(wqsrc + (size_t)kt * 524288 + (p - 4) * 128, w_voff, lds0 + pb + 32768 + (w * 4 + 2 * (p - 4)) * 512);
    else if (ROLE == 1) glds1(wasrc + kt * 65536, a_voff, lds0 + pb + 49152 + (w - 4) * 1024);
}

template <int PART>
DI void kv_proj(char* lds_raw, int b, int w, int h, const char* xsrc, const char* wqsrc, const char* wasrc, bf16x8 (&kvo)[16], unsigned tok) {
    const unsigned lds0 = (unsigned)(uintptr_t)lds_raw;
    const ArgP Ap = args();
    const int lane = lane_id(), kb = (b >> 3) & 7;
    const int r32 = lane & 31, hh = lane >> 5, kh = (w >> 1) & 1, ch = w & 1;
    const unsigned c_voff0 = (lane >> 3) * 1024 + (((lane & 7) ^ ((lane >> 4) & 7)) << 4), c_voff1 = c_voff0 ^ 64;
    const int c16 = lane & 15, g4 = lane >> 4;
    const unsigned w_voff = ((lane >> 2) & 7) * 8192 + (lane >> 5) * 64 + (((lane & 3) ^ (2 * (w & 1))) << 4);
    const char* csrc = Ap->ckv + (size_t)(256 * kb + 32 * w) * 1024;
    const char* wksrc = Ap->wuk + (size_t)(8 * w) * 8192 + h * 256;
    const char* wvsrc = Ap->wuv + (size_t)(8 * w) * 8192 + h * 256;
#define K1_PIECE(kt, p) do { \
        if ((p) < 4) glds1(csrc + (p) * 8192 + (kt) * 128, ((p) & 1) ? c_voff1 : c_voff0, lds0 + ((kt) & 1) * 65536 + w * 4096 + (p) * 1024); \
        else glds1((((p) < 6) ? wksrc : wvsrc) + (size_t)(kt) * 524288 + ((p) & 1) * 128, w_voff, lds0 + ((kt) & 1) * 65536 + 32768 + (w * 8 + 2 * ((p) - 4)) * 512); } while (0)
#define QP_NEXT(kt, p) qp_piece<PART>(kt, p, xsrc, wqsrc, wasrc, lds0, w, c_voff0, w_voff, ((lane_id() >> 5) * 8 + ((lane_id() >> 2) & 7)) * 1024 + (lane_id() & 3) * 16)
#pragma unroll
    for (int p = 0; p < 8; ++p) K1_PIECE(0, p);
    const bool mini = b < 64;
    bf16x8 xf0 = bf16x8{}, xf1 = bf16x8{}, xf2 = bf16x8{}, xf3 = bf16x8{};
    if (mini) {
        asm volatile("global_load_dwordx4 %0, %4, %5\n\tglobal_load_dwordx4 %1, %4, %5 offset:32\n\tglobal_load_dwordx4 %2, %4, %5 offset:64\n\tglobal_load_dwordx4 %3, %4, %5 offset:96"
                     : "+v"(xf0), "+v"(xf1), "+v"(xf2), "+v"(xf3) : "v"(r32 * 1024 + hh * 16), "s"(Ap->x + (size_t)(32 * b) * 1024 + w * 128) : "memory");
        glds2(Ap->wk + w * 2048, lane * 16, lds0 + 131072 + w * 4096);
        glds2(Ap->wv + w * 2048, lane * 16, lds0 + 131072 + w * 4096 + 2048);
    }
#pragma unroll
    for (int p = 0; p < 8; ++p) K1_PIECE(1, p);
    if (mini) {
        asm volatile("s_waitcnt vmcnt(8)" : "+v"(xf0), "+v"(xf1), "+v"(xf2), "+v"(xf3)::"memory");
        lds_cptr wp = (lds_cptr)lds_raw + 131072 + w * 4096 + (g4 & 1) * 2048 + (8 * hh + (c16 >> 2)) * 32 + (lane & 3) * 8;
        const bf16x8 wc0 = lds_tr2(wp, wp + 128), wc1 = lds_tr2(wp + 512, wp + 640), wc2 = lds_tr2(wp + 1024, wp + 1152), wc3 = lds_tr2(wp + 1536, wp + 1664);
        f32x16 d1 = MFMA(wc0, xf0, f32x16{}), d = MFMA(xf0, wc0, f32x16{});
        d1 = MFMA(wc1, xf1, d1); d = MFMA(xf1, wc1, d); d1 = MFMA(wc2, xf2, d1); d = MFMA(xf2, wc2, d); d1 = MFMA(wc3, xf3, d1); d = MFMA(xf3, wc3, d);
        lds_ptr rp = (lds_ptr)lds_raw + 131072 + w * 4096;
        lds_st_f4(rp + lane * 16, f32x4{d1[0], d1[1], d1[2], d1[3]});
        lds_st_f4(rp + 1024 + lane * 16, f32x4{d1[4], d1[5], d1[6], d1[7]});
        if (r32 >= 16) {
#pragma unroll
            for (int rq = 0; rq < 4; ++rq) lds_st_f4(rp + 2048 + rq * 512 + (hh * 16 + c16) * 16, f32x4{d[4 * rq], d[4 * rq + 1], d[4 * rq + 2], d[4 * rq + 3]});
        }
    }
    bf16x8 gx, gw;
    lds_cptr cA[2][2], wA[2][2];
#pragma unroll
    for (int sl = 0; sl < 2; ++sl)
#pragma unroll
        for (int s = 0; s < 2; ++s) {
            unsigned t = lds0 + sl * 65536 + (128 * kh + c16) * 128 + (((4 * s + g4) ^ ((c16 >> 1) & 7)) << 4); PIN(t); cA[sl][s] = (lds_cptr)(uintptr_t)t;
            unsigned u = lds0 + sl * 65536 + 32768 + g4 * 4096 + (4 * PART + 2 * ch) * 512 + (c16 >> 2) * 64 + ((s ^ (g4 & 1)) << 5) + (lane & 3) * 8; PIN(u);
            wA[sl][s] = (lds_cptr)(uintptr_t)u;
        }
    f32x4 acc[8][4];
#pragma unroll
    for (int i = 0; i < 8; ++i)
#pragma unroll
        for (int c = 0; c < 4; ++c) acc[i][c] = f32x4{};
    bf16x8 cf[4], wf[2][4];
#define K1_RDC(i, slot, s) cf[i] = lds_b128(cA[slot][(s) >> 1] + (4 * ((s) & 1) + (i)) * 2048)
#define K1_RDW(slot, s) do { _Pragma("unroll") for (int c = 0; c < 4; ++c) \
            wf[(s) >> 1][c] = lds_tr2(wA[slot][c & 1] + ((s) >> 1) * 16384 + (c >> 1) * 512, wA[slot][c & 1] + ((s) >> 1) * 16384 + (c >> 1) * 512 + 256); } while (0)
    unsigned tl = 0;
    TL(tl, 0);
    WAIT_BAR(8);
    TL(tl, 1);
    STAMP(args()->ws, 1);
    K1_RDW(0, 0);
    K1_RDC(0, 0, 0); K1_RDC(1, 0, 0); K1_RDC(2, 0, 0); K1_RDC(3, 0, 0);
#pragma unroll
    for (int kt = 0; kt < 8; ++kt) {
#pragma unroll
        for (int s = 0; s < 4; ++s) {
            if (s == 1) { K1_RDW(kt & 1, 2); }
            else if (s == 3) {
                SBAR();
                TL(tl, 2 + 2 * kt);
                if ((kt == WARM_KT || kt == 4) && !mini && WARM2) { WAIT_BAR(1); } else if (kt < 7) { WAIT_BAR(0); } else { LGKM_BAR(); }
                TL(tl, 3 + 2 * kt);
                if (kt < 7) K1_RDW((kt + 1) & 1, 0);
                if (PART == 0 && kt == 3 && w == 0 && lane_id() == 0) flag_set(args()->ws, FL_P1 + b, tok);
            }
            SBAR();
#pragma unroll
            for (int i = 0; i < 4; ++i) {
#pragma unroll
                for (int c = 0; c < 4; ++c) {
                    f32x4& d = acc[4 * (s & 1) + i][c];
                    d = PART == 0 ? MFMA16(wf[s >> 1][c], cf[i], d) : MFMA16(cf[i], wf[s >> 1][c], d);
                    PIN(d);
                    if (kt == 7 && s == 3 && (c & 1) && 2 * i + (c >> 1) < 7) { QP_NEXT(1, 2 * i + (c >> 1)); SBAR(); }
                }
                if (s < 3) K1_RDC(i, kt & 1, s + 1); else if (kt < 7) K1_RDC(i, (kt + 1) & 1, 0);
                if (s == 3 && kt + 2 < 8) K1_PIECE(kt + 2, i);
                if (s == 0 && kt >= 1 && kt + 1 < 8) K1_PIECE(kt + 1, 4 + i);
                if (kt == 6 && s == 3) QP_NEXT(0, i);
                if (kt == 7 && s == 0 && i < 3) QP_NEXT(0, 4 + i);
                SBAR();
            }
            if ((kt == WARM_KT || kt == 4) && s == 1 && !mini && WARM2) {
                const int n0 = ((b >> 3) - 8) * 64 + (b & 7) * 8 + w, n = n0 < (kt == 4 ? 832 : 1280) ? n0 : n0 - 768;
                const char *p0 = kt == 4 ? args()->x : args()->ckv, *p1 = kt == 4 ? args()->wqB : args()->wuk, *p2 = kt == 4 ? args()->wqA : args()->wuv;
                asm volatile("" : "+s"(p0), "+s"(p1), "+s"(p2));
                const char* src = n < 256 ? p0 + (size_t)n * 8192 : n < 768 ? p1 + (size_t)(n - 256) * 8192 : p2 + (size_t)(n - 768) * 8192;
                touch(uptr(src), lane_id() * 128, lds0 + SCR);
                SBAR();
            }
            if (kt == 1 && s == 2) {
                asm volatile("global_load_dwordx4 %0, %2, %3\n\tglobal_load_dwordx4 %1, %2, %4" : "=&v"(gx), "=&v"(gw)
                             : "v"(lane_id() * 16), "s"(args()->x + (size_t)(8 * b + w) * 1024), "s"(args()->gate_w) : "memory");
                SBAR();
            }
            if (kt == 2 && s == 0) {
                asm volatile("" : "+v"(gx), "+v"(gw));
                float gs = wave_sum63(dot8(gx, gw, 0.f));
                gs = 1.f / (1.f + __expf(-gs));
                if (lane_id() == 63) asm volatile("global_store_dword %0, %1, %2 sc1" ::"v"(0), "v"(gs), "s"(args()->ws + WS_GATE + (8 * b + w) * 4) : "memory");
                SBAR();
            }
            if (PART == 0 && kt == 0 && s == 0 && mini && w < 3) {
                const int lane = lane_id(), r32 = lane & 31, hh = lane >> 5;
                const char* dst = args()->ws + WS_KVA + (size_t)(b >> 1) * 6144;
                lds_cptr sp = (lds_cptr)lds_raw + 131072;
                int o0, o1;
                if (w == 0) { o0 = lane * 16; o1 = 1024 + lane * 16; dst += (b & 1) * 1024; }
                else { const int idx = hh * 16 + (r32 & 15); o0 = 2048 + (2 * (w - 1)) * 512 + idx * 16; o1 = o0 + 512; dst += 2048 + (2 * (b & 1) + (w - 1)) * 1024; }
                f32x4 a = f32x4{0.f, 0.f, 0.f, 0.f}, c = a;
#pragma unroll
                for (int u = 0; u < 8; ++u) { a += lds_f4(sp + u * 4096 + o0); c += lds_f4(sp + u * 4096 + o1); }
                u32x4 pk = {cvtpk(a[0], a[1]), cvtpk(a[2], a[3]), cvtpk(c[0], c[1]), cvtpk(c[2], c[3])};
                if (w != 0 && r32 >= 16) { const unsigned one = (r32 == 16 || r32 == 20) ? 0x3F803F80u : 0u; pk = u32x4{one, one, one, one}; }
                gstore<0>(dst, lane * 16, __builtin_bit_cast(bf16x8, pk));
                SBAR();
            }
        }
    }
    STAMP(args()->ws, 2);
    TLOUT(tl, 0);
#pragma unroll
    for (int n = 0; n < 16; ++n) {
        kvo[n] = PART == 0 ? pack44(acc[n >> 1][2 * (n & 1)], acc[n >> 1][2 * (n & 1) + 1])
                           : pack44(acc[2 * (n >> 2)][n & 3], acc[2 * (n >> 2) + 1][n & 3]);
        PIN(kvo[n]);
    }
#undef K1_RDC
#undef K1_RDW
#undef K1_PIECE
#undef QP_NEXT
}

DI void b_slow(const char* kvh, int lane, const bf16x8 (&q)[8], f32x4 (&o)[8][2], float (&l)[2], int qrow0, int ntile) {
    const int c16 = lane & 15, g4 = lane >> 4;
    float m[2] = {-INFINITY, -INFINITY};
    l[0] = 0.f; l[1] = 0.f;
#pragma unroll
    for (int db = 0; db < 8; ++db) { o[db][0] = f32x4{}; o[db][1] = f32x4{}; }
#pragma unroll 1
    for (int t = 0; t < ntile; ++t) {
        const char* tp = kvh + (size_t)t * 32768 + lane * 16;
        f32x4 sc[4][2];
#pragma unroll
        for (int kb = 0; kb < 4; ++kb) {
            sc[kb][0] = f32x4{}; sc[kb][1] = f32x4{};
#pragma unroll
            for (int ds = 0; ds < 4; ++ds) {
                const bf16x8 kf = *(const bf16x8*)(tp + (kb * 4 + ds) * 1024);
                sc[kb][0] = MFMA16(kf, q[ds], sc[kb][0]); sc[kb][1] = MFMA16(kf, q[4 + ds], sc[kb][1]);
            }
        }
        bf16x8 pk[2][2];
        float al[2];
#pragma unroll
        for (int qh = 0; qh < 2; ++qh) {
            const int dq = qrow0 + 16 * qh + c16 - 64 * t - 4 * g4;
#pragma unroll
            for (int kb = 0; kb < 4; ++kb)
#pragma unroll
                for (int r = 0; r < 4; ++r) if (16 * kb + r > dq) sc[kb][qh][r] = -INFINITY;
            const float mn = fmaxf(m[qh], grp_max(max16(sc[0][qh], sc[1][qh], sc[2][qh], sc[3][qh])));
            const float ms = (mn == -INFINITY) ? 0.f : mn;
            al[qh] = ex2(m[qh] - ms);
            m[qh] = mn;
            float a0 = 0.f;
#pragma unroll
            for (int kb = 0; kb < 4; ++kb)
#pragma unroll
                for (int r = 0; r < 4; ++r) { sc[kb][qh][r] = ex2(sc[kb][qh][r] - ms); a0 += sc[kb][qh][r]; }
            l[qh] = l[qh] * al[qh] + a0;
            pk[0][qh] = pack44(sc[0][qh], sc[1][qh]); pk[1][qh] = pack44(sc[2][qh], sc[3][qh]);
        }
#pragma unroll
        for (int db = 0; db < 8; ++db) {
            o[db][0] *= al[0]; o[db][1] *= al[1];
#pragma unroll
            for (int kh = 0; kh < 2; ++kh) {
                const bf16x8 vf = *(const bf16x8*)(tp + 16384 + (kh * 8 + db) * 1024);
                o[db][0] = MFMA16(vf, pk[kh][0], o[db][0]); o[db][1] = MFMA16(vf, pk[kh][1], o[db][1]);
            }
        }
    }
}

DI void q_slow(const char* xrows, const char* wq, int lane, bf16x8 (&q)[8]) {
    const int c16 = lane & 15, g4 = lane >> 4;
    f32x4 acc[8][2];
#pragma unroll
    for (int c = 0; c < 8; ++c) { acc[c][0] = f32x4{}; acc[c][1] = f32x4{}; }
#pragma unroll 1
    for (int ks = 0; ks < 16; ++ks) {
        const bf16x8 x0 = *(const bf16x8*)(xrows + c16 * 1024 + ks * 64 + g4 * 16), x1 = *(const bf16x8*)(xrows + (16 + c16) * 1024 + ks * 64 + g4 * 16);
#pragma unroll
        for (int c = 0; c < 8; ++c) {
            bf16x8 wf;
#pragma unroll
            for (int e = 0; e < 8; ++e) wf[e] = *(const short*)(wq + (size_t)(32 * ks + 8 * g4 + e) * 8192 + (16 * c + c16) * 2);
            acc[c][0] = MFMA16(wf, x0, acc[c][0]); acc[c][1] = MFMA16(wf, x1, acc[c][1]);
        }
    }
#pragma unroll
    for (int ds = 0; ds < 4; ++ds) { q[ds] = pack44s(acc[2 * ds][0], acc[2 * ds + 1][0], QSCALE_B); q[4 + ds] = pack44s(acc[2 * ds][1], acc[2 * ds + 1][1], QSCALE_B); }
}

DI void a_slow(const char* kva, int lane, bf16x8 q, f32x16& o) {
    float m = -INFINITY;
    o = f32x16{};
#pragma unroll 1
    for (int t = 0; t < 32; ++t) {
        const char* tp = kva + (size_t)t * 6144 + lane * 16;
        f32x16 s0 = MFMA(*(const bf16x8*)(tp), q, f32x16{});
        f32x16 s1 = MFMA(*(const bf16x8*)(tp + 1024), q, f32x16{});
        const float mn = fmaxf(m, rowmax32(s0, s1));
        const float ms = (mn == -INFINITY) ? 0.f : mn;
        const float al = ex2(m - ms);
        m = mn;
#pragma unroll
        for (int r = 0; r < 16; ++r) { s0[r] = ex2(s0[r] - ms); s1[r] = ex2(s1[r] - ms); o[r] *= al; }
        o = MFMA(*(const bf16x8*)(tp + 2048), pack8<0>(s0), o);
        o = MFMA(*(const bf16x8*)(tp + 3072), pack8<1>(s0), o);
        o = MFMA(*(const bf16x8*)(tp + 4096), pack8<0>(s1), o);
        o = MFMA(*(const bf16x8*)(tp + 5120), pack8<1>(s1), o);
    }
}

template <int ROLE>
DI void q_proj(char* lds_raw, char* ws, f32x4 (&acc)[8][2], f32x16& acca, const char* xsrc, const char* wqsrc, const char* wasrc, int w, int h,
               const char* kvw, int t2, int kb, const bf16x8 (&kvo)[16], unsigned tok) {
    constexpr int NACC = 4 + ROLE;
    const unsigned lds0 = (unsigned)(uintptr_t)lds_raw;
    const int lane = lane_id(), r32 = lane & 31, hh = lane >> 5, c16 = lane & 15, g4 = lane >> 4;
    const unsigned x_voff = (lane >> 3) * 1024 + (((lane & 7) ^ ((lane >> 4) & 7)) << 4);
    const unsigned w_voff = ((lane >> 2) & 7) * 8192 + (lane >> 5) * 64 + (((lane & 3) ^ (2 * (w & 1))) << 4);
    const unsigned a_voff = ((lane >> 5) * 8 + ((lane >> 2) & 7)) * 1024 + (lane & 3) * 16;
    lds_cptr xA[2][2], wA[2][2], aA[2], yA[4];
#pragma unroll
    for (int sl = 0; sl < 2; ++sl) {
#pragma unroll
        for (int s = 0; s < 2; ++s) {
            unsigned t = lds0 + 2 * sl * PSTG + (32 * w + c16) * 128 + (((4 * s + g4) ^ ((c16 >> 1) & 7)) << 4);
            unsigned u = lds0 + 2 * sl * PSTG + 32768 + g4 * 2048 + (c16 >> 2) * 64 + ((s ^ (g4 & 1)) << 5) + (lane & 3) * 8;
            PIN(t); PIN(u); xA[sl][s] = (lds_cptr)(uintptr_t)t; wA[sl][s] = (lds_cptr)(uintptr_t)u;
        }
        unsigned u = lds0 + 2 * sl * PSTG + 49152 + hh * 512 + (c16 >> 2) * 64 + (g4 & 1) * 32 + (lane & 3) * 8;
        if (ROLE == 1) PIN(u);
        aA[sl] = (lds_cptr)(uintptr_t)u;
    }
#pragma unroll
    for (int s = 0; s < 4; ++s) {
        unsigned t = lds0 + (32 * w + r32) * 128 + (((2 * s + hh) ^ ((r32 >> 1) & 7)) << 4);
        if (ROLE == 1) PIN(t);
        yA[s] = (lds_cptr)(uintptr_t)t;
    }
    bf16x8 xf[2][2], wf[2][4], yf, af;
    const char* kvd = ws + WS_KVB + (size_t)(h * 32 + 4 * kb + 2 * ((w >> 1) & 1)) * 32768 + (ROLE == 0 ? 2 * (w & 1) : 16 + 4 * (w & 1)) * 1024;
#define KV_STORE(n) do { \
        if (ROLE == 0) gstore<((n) & 1) * 1024>(kvd + ((n) >> 3) * 32768 + (((n) >> 1) & 3) * 4096, lane * 16, kvo[n]); \
        else gstore<((n) & 3) * 1024>(kvd + ((n) >> 3) * 32768 + (((n) >> 2) & 1) * 8192, lane * 16, kvo[n]); } while (0)
#define KV_PIECE(n) do { const int u_ = (n) >> 2; \
        glds1s(kvw + (u_ == 0 ? 0 : u_ == 1 ? 32768 : u_ == 2 ? 16384 : u_ == 3 ? (size_t)t2 * 32768 : 49152) + ((n) & 3) * 1024, lane * 16, \
               lds0 + (u_ == 0 ? KS0 : u_ == 1 ? KS1 : u_ == 2 ? VS0 : u_ == 3 ? KS2 : VS1) + w * 4096 + ((n) & 3) * 1024); } while (0)
#define QP_RD(buf, kt, s) do { const int sg_ = ((kt) % 3) >> 1, so_ = (((kt) % 3) & 1) * PSTG; \
        if (((s) & 1) == 0) { xf[(s) >> 1][0] = lds_b128(xA[sg_][(s) >> 1] + so_); xf[(s) >> 1][1] = lds_b128(xA[sg_][(s) >> 1] + so_ + 2048); } \
        _Pragma("unroll") for (int c = 0; c < 4; ++c) { const lds_cptr p_ = wA[sg_][c & 1] + so_ + ((s) >> 1) * 8192 + (2 * ((s) & 1) + (c >> 1)) * 512; wf[buf][c] = lds_tr2(p_, p_ + 256); } } while (0)
#define QP_RDA(kt, s) do { const int sg_ = ((kt) % 3) >> 1, so_ = (((kt) % 3) & 1) * PSTG; \
        yf = lds_b128(yA[s] + ((kt) % 3) * PSTG); af = lds_tr2(aA[sg_] + so_ + (s) * 1024, aA[sg_] + so_ + (s) * 1024 + 256); } while (0)
    if (ROLE == 1) __builtin_amdgcn_s_setprio(QP_APRIO);
#pragma unroll
    for (int c = 0; c < 8; ++c) { acc[c][0] = f32x4{}; acc[c][1] = f32x4{}; }
    acca = f32x16{};
    unsigned tl = 0;
    TL(tl, 0);
    if (ROLE == 1) { WAIT_BAR(7); } else { WAIT_BAR(6); }
    TL(tl, 1);
    STAMP(ws, 3);
    QP_RD(0, 0, 0);
    const bool urgent = kb < 2;
#define QP_TILE(kt) do { \
    _Pragma("unroll") for (int s = 0; s < 4; ++s) { \
        if (ROLE == 1) QP_RDA(kt, s); \
        if (s < 3) { QP_RD((s + 1) & 1, kt, s + 1); } \
        else if ((kt) < 7) { \
            SBAR(); \
            if (ROLE == 1 && (kt) == 5 && w == 4) { flag_wait<1, 1>(ws, FL_KV + 32 * h, tok); flag_wait<4, 63>(ws, FL_P1, tok); }     \
            TL(tl, 2 + 2 * (kt)); \
            if ((kt) == 6) { if (ROLE == 1) { WAIT_BAR(0); } else { WAIT_BAR(9); } } \
            else if ((kt) >= 1 && (kt) <= 2 && urgent) { if (ROLE == 1) { WAIT_BAR(15); } else { WAIT_BAR(14); } } \
            else if ((kt) >= 3 && !urgent) { if (ROLE == 1) { WAIT_BAR(10); } else { WAIT_BAR(9); } } \
            else { if (ROLE == 1) { WAIT_BAR(7); } else { WAIT_BAR(6); } } \
            TL(tl, 3 + 2 * (kt)); \
            QP_RD(0, (kt) + 1, 0); \
            if (ROLE == 0 && (kt) == 3 && urgent && w == 0 && lane == 0) flag_set(ws, FL_KV + 32 * h + kb, tok);     \
        } \
        SBAR(); \
        _Pragma("unroll") for (int c = 0; c < NACC; ++c) { \
            if (c < 4) { f32x4 &d0 = acc[4 * (s & 1) + (c & 3)][0], &d1 = acc[4 * (s & 1) + (c & 3)][1]; \
                d0 = MFMA16(wf[s & 1][c & 3], xf[s >> 1][0], d0); PIN(d0); d1 = MFMA16(wf[s & 1][c & 3], xf[s >> 1][1], d1); PIN(d1); } \
            else { acca = MFMA(af, yf, acca); PIN(acca); } \
              \
            if (s < 3 && c < 3) { \
                if ((kt) + 2 < 8) { if (3 * s + c < 7) qp_piece<ROLE>((kt) + 2, 3 * s + c, xsrc, wqsrc, wasrc, lds0, w, x_voff, w_voff, a_voff); } \
                else if (ROLE == 0) { if ((kt) == 6) KV_PIECE(3 * s + c); else if (3 * s + c < 8) KV_PIECE(12 + 3 * s + c); } \
            } \
            if (ROLE == 0 && (kt) == 6 && s == 3 && c < 3) KV_PIECE(9 + c); \
            if (s == 3 && c == 0) { if ((kt) < 2 && urgent) { KV_STORE((8 * (kt) + 0) & 15); KV_STORE((8 * (kt) + 1) & 15); } \
                if ((kt) >= 2 && (kt) < 7 && 0 < ((kt) == 6 ? 4 : 3) && !urgent) KV_STORE((3 * ((kt) - 2) + 0 + 16) & 15); } \
            if (s == 3 && c == 1) { if ((kt) < 2 && urgent) { KV_STORE((8 * (kt) + 2) & 15); KV_STORE((8 * (kt) + 3) & 15); } \
                if ((kt) >= 2 && (kt) < 7 && 1 < ((kt) == 6 ? 4 : 3) && !urgent) KV_STORE((3 * ((kt) - 2) + 1 + 16) & 15); } \
            if (s == 3 && c == 2) { if ((kt) < 2 && urgent) { KV_STORE((8 * (kt) + 4) & 15); KV_STORE((8 * (kt) + 5) & 15); } \
                if ((kt) >= 2 && (kt) < 7 && 2 < ((kt) == 6 ? 4 : 3) && !urgent) KV_STORE((3 * ((kt) - 2) + 2 + 16) & 15); } \
            if (s == 3 && c == 3) { if ((kt) < 2 && urgent) { KV_STORE((8 * (kt) + 6) & 15); KV_STORE((8 * (kt) + 7) & 15); } \
                if ((kt) >= 2 && (kt) < 7 && 3 < ((kt) == 6 ? 4 : 3) && !urgent) KV_STORE((3 * ((kt) - 2) + 3 + 16) & 15); } \
            SBAR(); \
        } \
    } } while (0)
    QP_TILE(0); QP_TILE(1); QP_TILE(2); QP_TILE(3); QP_TILE(4); QP_TILE(5); QP_TILE(6); QP_TILE(7);
    TL(tl, 16); TL(tl, 17); TLOUT(tl, 1);
#undef QP_TILE
#undef KV_STORE
#undef KV_PIECE
#undef QP_RD
#undef QP_RDA
}

DI void out_proj(char* lds_raw, int b, int w, unsigned tok) {
    const ArgP Ap = args();
    const int lane = lane_id(), j = b >> 3, qt = 8 * ((b & 7) >> 1) + (j >> 2), nt = 4 * (b & 1) + (j & 3);
    const unsigned ring0 = (unsigned)(uintptr_t)lds_raw + w * 20480;
    const int c16 = lane & 15, g4 = lane >> 4;
    const char* c0 = Ap->ws + WS_CTX + (size_t)(4 * qt) * 144 * 1024;
    const unsigned w_voff = ((lane >> 2) & 7) * 1024 + (lane >> 5) * 64 + (((lane & 3) ^ ((lane >> 3) & 2)) << 4);
    lds_cptr wp[2];
#pragma unroll
    for (int hf = 0; hf < 2; ++hf) wp[hf] = (lds_cptr)lds_raw + w * 20480 + (g4 >> 1) * 1024 + (4 * (g4 & 1) + (c16 >> 2)) * 64 + ((hf ^ (g4 & 1)) << 5) + (lane & 3) * 8;
    bf16x8 cf[4][4];
    const int par = qt >> 4;
    const char* cE = c0 + (size_t)(16 + 4 * (4 * w + par)) * 1024;
    const char* cL = c0 + (size_t)(16 + 4 * (4 * w + 1 - par)) * 1024;
    const char* cA = c0 + (size_t)(2 * w) * 1024;
    const char* wE = Ap->woB + (size_t)(4 * (4 * w + par)) * 32768 + nt * 128;
    const char* wL = Ap->woB + (size_t)(4 * (4 * w + 1 - par)) * 32768 + nt * 128;
    const char* wAq = Ap->woA + (size_t)(2 * w) * 32768 + nt * 128;
#define K3_IDX(s) ((s) < 8 ? ((s) & 3) + 8 * ((s) >> 2) : (s) < 10 ? (s) - 8 : (((s) - 10) & 3) + 8 * (((s) - 10) >> 2))
#define K3_W(s) do { const unsigned d_ = ring0 + ((s) % 5) * 4096; const char* wsrc_ = ((s) < 8 ? wE : (s) < 10 ? wAq : wL) + K3_IDX(s) * 32768; \
        glds1(wsrc_, w_voff, d_); glds1(wsrc_ + 8192, w_voff, d_ + 1024); glds1(wsrc_ + 16384, w_voff, d_ + 2048); glds1(wsrc_ + 24576, w_voff, d_ + 3072); } while (0)
#define K3_C(s) do { const char* cs_ = ((s) < 8 ? cE : (s) < 10 ? cA : cL) + K3_IDX(s) * 1024; \
        asm volatile("global_load_dwordx4 %0, %4, %5 sc1\n\tglobal_load_dwordx4 %1, %4, %6 sc1\n\tglobal_load_dwordx4 %2, %4, %7 sc1\n\tglobal_load_dwordx4 %3, %4, %8 sc1" \
                     : "=&v"(cf[(s) & 3][0]), "=&v"(cf[(s) & 3][1]), "=&v"(cf[(s) & 3][2]), "=&v"(cf[(s) & 3][3]) \
                     : "v"(lane * 16), "s"(cs_), "s"(cs_ + 144 * 1024), "s"(cs_ + 288 * 1024), "s"(cs_ + 432 * 1024) : "memory"); } while (0)
#define K3_WAIT(s, N) asm volatile("s_waitcnt vmcnt(%4)" : "+v"(cf[(s) & 3][0]), "+v"(cf[(s) & 3][1]), "+v"(cf[(s) & 3][2]), "+v"(cf[(s) & 3][3]) : "i"(N) : "memory")
    LGKM_BAR();
    __builtin_amdgcn_s_setprio(0);
    if (w == 0) flag_wait<1, 63>(Ap->ws, FL_EARLY + 64 * (qt >> 1), tok);
#pragma unroll
    for (int s = 0; s < 4; ++s) K3_W(s);
    STAMP(Ap->ws, 8);
    asm volatile("s_barrier" ::: "memory");
    f32x4 acc[4][4];
#pragma unroll
    for (int nb = 0; nb < 4; ++nb)
#pragma unroll
        for (int qb = 0; qb < 4; ++qb) acc[nb][qb] = f32x4{};
#pragma unroll
    for (int s = 0; s < 4; ++s) K3_C(s);
#pragma unroll
    for (int s = 0; s < 18; ++s) {
        if (s == 8) {
            if (w == 0) flag_wait<2, 63>(Ap->ws, FL_LATE + 128 * (qt >> 1), tok);
            STAMP(Ap->ws, 11);
            asm volatile("s_barrier" ::: "memory");
#pragma unroll
            for (int u = 8; u < 12; ++u) K3_C(u);
        }
        K3_WAIT(s, s < 4 ? 12 + 4 * s : s < 8 ? 40 - 4 * s : s < 12 ? 4 * s - 20 : (s <= 14 ? 24 : 8 * (17 - s)));
#pragma unroll
        for (int nb = 0; nb < 4; ++nb) {
            const lds_cptr p_ = wp[nb & 1] + (s % 5) * 4096 + (nb >> 1) * 512;
            const bf16x8 wf = lds_tr2(p_, p_ + 2048);
#pragma unroll
            for (int qb = 0; qb < 4; ++qb) { acc[nb][qb] = MFMA16(wf, cf[s & 3][qb], acc[nb][qb]); PIN(acc[nb][qb]); }
        }
        SBAR();
        if (s + 4 < 18) { K3_W(s + 4); if (s < 4 || s >= 8) K3_C(s + 4); }
        SBAR();
    }
#undef K3_IDX
#undef K3_W
#undef K3_C
#undef K3_WAIT
    STAMP(Ap->ws, 9);
    lds_ptr rp = (lds_ptr)lds_raw + w * 20480 + lane * 16;
#pragma unroll
    for (int nb = 0; nb < 4; ++nb)
#pragma unroll
        for (int qb = 0; qb < 4; ++qb) lds_st_f4(rp + (nb * 4 + qb) * 1024, acc[nb][qb]);
    LGKM_BAR();
    {
        const int qb = w >> 1, nb = 2 * (w & 1) + (g4 >> 1), t = g4 & 1;
        lds_cptr sp = (lds_cptr)lds_raw + (nb * 4 + qb) * 1024 + (c16 + 32 * t) * 16;
        f32x4 x = f32x4{0.f, 0.f, 0.f, 0.f}, y = x;
#pragma unroll
        for (int u = 0; u < 8; ++u) { x += lds_f4(sp + u * 20480); y += lds_f4(sp + u * 20480 + 256); }
        *(bf16x8*)(Ap->out + (size_t)(64 * qt + 16 * qb + c16) * 1024 + (64 * nt + 16 * nb + 8 * t) * 2) = pack44(x, y);
    }
    STAMP(Ap->ws, 10);
}

__global__ __launch_bounds__(512, 2) void gated_mix_fused(const Args A_) {
    (void)A_;
    __shared__ __attribute__((aligned(16))) char lds[163840];
    const int w = rfl(threadIdx.x >> 6);
    const int b = blockIdx.x, j = b >> 3, h = (b & 7) + 8 * (j >> 3), i = j & 7;
    const int qf = (h & 1) ? 15 - i : i, qs = 15 - qf;
    const int n1 = 2 * qf + 2;
    const unsigned lds0 = (unsigned)(uintptr_t)lds;
    unsigned tok = launch_token();
    asm volatile("" : "+s"(tok));
    const char* wqsrc = args()->wqB + (size_t)(8 * w) * 8192 + h * 256;
#define GATE(blk_, wv_, row_) (*(const __attribute__((address_space(3))) float*)((lds_cptr)lds + GTAB + (blk_) * 512 + (wv_) * 128 + (row_) * 4))
    STAMP(args()->ws, 0);
    if ((j >> 1) == 4) {
        unsigned long long pc; unsigned len;
        asm volatile("s_getpc_b64 %0\n\ts_mov_b32 %1, gm_code_end - .\n" : "=s"(pc), "=s"(len));
        unsigned off = (((j & 1) * 8 + w) * 64 + lane_id()) * 128;
        off = off < len ? off : 0u;
        touch((const char*)pc, off, lds0 + SCR);
    }
    if (w < 4) {
        const char* xsrc = args()->x + (size_t)(128 * qf + 32 * w) * 1024;
        bf16x8 kvo[16];
        kv_proj<0>(lds, b, w, h, xsrc, wqsrc, nullptr, kvo, tok);
        char* const ws = args()->ws;
        const char* kvh = ws + WS_KVB + (size_t)h * 32 * 32768;
        const char* ctx = ws + WS_CTX;
        const int lane = lane_id();
        if (w == 0 && lane < 2) *(__attribute__((address_space(3))) unsigned*)((lds_ptr)lds + CNT + 4 * lane) = 0u;
        const char* kvw = kvh + w * 4096;
        const unsigned cnt_addr = lds0 + CNT, one = 1u;
        unsigned pollv = 0;
        int g = -1;
#define ARRIVE(VM, LG) asm volatile("s_waitcnt vmcnt(" #VM ") lgkmcnt(" #LG ")\n\ts_mov_b64 exec, 1\n\tds_add_u32 %0, %1\n\ts_mov_b64 exec, -1" ::"v"(cnt_addr), "v"(one) : "memory")
#define POLL(off) do { pollv = *(volatile __attribute__((address_space(3))) unsigned*)(uintptr_t)(cnt_addr + (off)); } while (0)
#define AWAIT_(off, target) do { unsigned sv_; asm volatile("v_readfirstlane_b32 %0, %1\n\ts_nop 0\n\ts_cmp_ge_u32 %0, %3\n\ts_cbranch_scc1 2f\n" \
            "1:\n\ts_sleep 1\n\tds_read_b32 %1, %2 offset:" #off "\n\ts_waitcnt lgkmcnt(0)\n\tv_readfirstlane_b32 %0, %1\n\ts_nop 0\n\ts_cmp_ge_u32 %0, %3\n\ts_cbranch_scc0 1b\n2:" \
            : "=&s"(sv_), "+v"(pollv) : "v"(cnt_addr), "s"(target) : "memory", "scc"); } while (0)
#define AWAIT() AWAIT_(0, 4 * g + 8)
        bf16x8 q[8];
        {
            f32x4 acc[8][2];
            f32x16 acca;
            q_proj<0>(lds, ws, acc, acca, xsrc, wqsrc, nullptr, w, h, kvw, 2 < n1 ? 2 : 0, i, kvo, tok);
            WAIT_BAR(8);
            if (i >= 2 && w == 0 && lane == 0) flag_set(ws, FL_KV + 32 * h + i, tok);
            __builtin_amdgcn_s_setprio(B_PRIO);
            ARRIVE(16, 0);
#pragma unroll
            for (int ds = 0; ds < 4; ++ds) { q[ds] = pack44s(acc[2 * ds][0], acc[2 * ds + 1][0], QSCALE_B); q[4 + ds] = pack44s(acc[2 * ds][1], acc[2 * ds + 1][1], QSCALE_B); }
        }
        STAMP(ws, 4);
        POLL(0); AWAIT();
        g = 0;
        int kp = KS2, kc = KS0, kn = KS1, vq = VS1, vo = VS2, vp = VS3, vc = VS0;
        const char* ksrc = kvw + (size_t)(2 < n1 ? 2 : 0) * 32768;
        const char* vsrc = kvw + 49152;
#define ROT() do { const int t0_ = kp; kp = kc; kc = kn; kn = t0_; const int t1_ = vq; vq = vo; vo = vp; vp = vc; vc = t1_; \
            vsrc = ksrc + 16384; ksrc = (g + 3 == n1) ? kvw : ksrc + 32768; ++g; KVWAIT(); } while (0)
#define KVWAIT() do { unsigned sn_; asm volatile("s_cmp_lg_u32 %2, 6\n\ts_cbranch_scc1 3f\n\ts_mov_b32 %1, 0\n" \
            "1:\n\tv_mbcnt_lo_u32_b32 %0, -1, 0\n\tv_and_b32 %0, 7, %0\n\tv_lshlrev_b32 %0, 2, %0\n\tglobal_load_dword %0, %0, %3 sc1\n\ts_waitcnt vmcnt(0)\n\tv_cmp_ne_u32 vcc, %5, %0\n\t" \
            "s_add_u32 %1, %1, 1\n\ts_cmp_gt_u32 %1, %4\n\ts_cbranch_scc1 3f\n\ts_cbranch_vccz 3f\n\ts_sleep 2\n\ts_branch 1b\n3:" \
            : "+v"(pollv), "=&s"(sn_) : "s"(g), "s"(ws + WS_FLAG + h * 128), "s"(SPIN_CAP), "s"(tok) : "memory", "scc", "vcc"); } while (0)
        bf16x8 fr[8];
        f32x4 sc[4][2], o[8][2], negm[2];
        unsigned pw[16];
        float ls[2] = {0.f, 0.f};
#pragma unroll
        for (int db = 0; db < 8; ++db) { o[db][0] = f32x4{}; o[db][1] = f32x4{}; }
#define LADDR(off) ({ unsigned a_ = lds0 + lane * 16 + (off); PIN(a_); (lds_cptr)(uintptr_t)a_; })
#define DMA4(src, dst) do { setm0(lds0 + (dst) + w * 4096); gl<0>(src, lane * 16); gl<1024>(src, lane * 16); gl<2048>(src, lane * 16); gl<3072>(src, lane * 16); } while (0)
#define DMA1(n, src, dst) do { if ((n) == 0) setm0(lds0 + (dst) + w * 4096); gl<(n) * 1024>(src, lane * 16); } while (0)
#define PKF(kh, qh) __builtin_bit_cast(bf16x8, u32x4{pw[8 * (kh) + 4 * (qh)], pw[8 * (kh) + 4 * (qh) + 1], pw[8 * (kh) + 4 * (qh) + 2], pw[8 * (kh) + 4 * (qh) + 3]})
#define SCP(kh, j) sc[2 * (kh) + (((j) >> 1) & 1)][(j) >> 2]
#define FIN(j, kh) do { pw[8 * (kh) + (j)] = cvtpk(SCP(kh, j)[2 * ((j) & 1)], SCP(kh, j)[2 * ((j) & 1) + 1]); \
            ls[(j) >> 2] += SCP(kh, j)[2 * ((j) & 1)]; ls[(j) >> 2] += SCP(kh, j)[2 * ((j) & 1) + 1]; PIN(ls[(j) >> 2]); PIN(pw[8 * (kh) + (j)]); } while (0)
#define EXP2(kh, j) do { SCP(kh, j)[2 * ((j) & 1)] = ex2(SCP(kh, j)[2 * ((j) & 1)]); SCP(kh, j)[2 * ((j) & 1) + 1] = ex2(SCP(kh, j)[2 * ((j) & 1) + 1]); PIN(SCP(kh, j)); } while (0)
#define QK_GAP(m, ZERO, HASP, KB, NB, DK, EARLY) do { \
            sc[(m) >> 2][0] = MFMA16(fr[(m) & 7], q[(m) & 3], ((m) & 3) ? sc[(m) >> 2][0] : (ZERO) ? f32x4{} : negm[0]); PIN(sc[(m) >> 2][0]); \
            sc[(m) >> 2][1] = MFMA16(fr[(m) & 7], q[4 + ((m) & 3)], ((m) & 3) ? sc[(m) >> 2][1] : (ZERO) ? f32x4{} : negm[1]); PIN(sc[(m) >> 2][1]); \
            if ((m) < 8) fr[(m) & 7] = lds_b128((KB) + ((m) + 8) * 1024); else if (HASP) fr[(m) & 7] = lds_b128((NB) + ((m) - 8) * 1024); \
            if ((HASP) && (m) < 8) FIN((m) & 7, 1); \
            if ((EARLY) && (m) >= 10) EXP2(0, ((m) - 10) & 7); \
            if ((DK) && ((m) & 1)) { if ((m) < 8) DMA1(((m) >> 1) & 3, ksrc, kp); else DMA1(((m) >> 1) & 3, vsrc, vq); } \
            SBAR(); } while (0)
#define PV_GAP(m, VB, NB, EXPS, SYNC) do { \
            o[(m) & 7][0] = MFMA16(fr[(m) & 7], PKF((m) >> 3, 0), o[(m) & 7][0]); PIN(o[(m) & 7][0]); \
            o[(m) & 7][1] = MFMA16(fr[(m) & 7], PKF((m) >> 3, 1), o[(m) & 7][1]); PIN(o[(m) & 7][1]); \
            if ((SYNC) && (m) == 8) AWAIT(); \
            if ((m) < 8) fr[(m) & 7] = lds_b128((VB) + ((m) + 8) * 1024); else if (SYNC) fr[(m) & 7] = lds_b128((NB) + ((m) - 8) * 1024); \
            if ((SYNC) && (m) == 4) POLL(0); \
            if ((EXPS) == 1) { if ((m) >= 8) FIN(((m) - 8) & 7, 0); \
                               if ((m) < 2) EXP2(0, ((m) + 6) & 7); else if ((m) < 10) EXP2(1, ((m) - 2) & 7); } \
            if ((EXPS) == 3) { if ((m) >= 8) FIN(((m) - 8) & 7, 0); \
                               if ((m) < 8) EXP2(0, (m) & 7); else EXP2(1, (m) & 7); } \
            if ((EXPS) == 2) { \
                if ((m) == 1 || (m) == 2) ROWREF((m) - 1); \
                if ((m) >= 4 && (m) < 12) { SUBEXP(((m) - 4) >> 1 & 3, (m) & 1); } } \
            SBAR(); } while (0)
#define ROWREF(qh) do { const float m_ = grp_max(max16(sc[0][qh], sc[1][qh], sc[2][qh], sc[3][qh])); negm[qh] = f32x4{-m_, -m_, -m_, -m_}; PIN(negm[qh]); } while (0)
#define SUBEXP(kb, qh) do { _Pragma("unroll") for (int e = 0; e < 4; ++e) sc[kb][qh][e] = ex2(sc[kb][qh][e] + negm[qh][0]); PIN(sc[kb][qh]); } while (0)
#define ALL16(M, ...) do { M(0, __VA_ARGS__); M(1, __VA_ARGS__); M(2, __VA_ARGS__); M(3, __VA_ARGS__); M(4, __VA_ARGS__); M(5, __VA_ARGS__); M(6, __VA_ARGS__); M(7, __VA_ARGS__); \
            M(8, __VA_ARGS__); M(9, __VA_ARGS__); M(10, __VA_ARGS__); M(11, __VA_ARGS__); M(12, __VA_ARGS__); M(13, __VA_ARGS__); M(14, __VA_ARGS__); M(15, __VA_ARGS__); } while (0)
#define ALL8(M, ...) do { M(0, __VA_ARGS__); M(1, __VA_ARGS__); M(2, __VA_ARGS__); M(3, __VA_ARGS__); M(4, __VA_ARGS__); M(5, __VA_ARGS__); M(6, __VA_ARGS__); M(7, __VA_ARGS__); } while (0)
#define DIAG_MASK(C) do { _Pragma("unroll") for (int e = 0; e < 4; ++e) C[e] = fminf(C[e], __uint_as_float(((unsigned)(dd_ - e) & 0x80000000u) | 0x7F800000u)); } while (0)
#define NEG4 f32x4{-INFINITY, -INFINITY, -INFINITY, -INFINITY}
#define MASK_TILE() do { const int ln_ = lane_id(), dd_ = (ln_ & 15) - 4 * (ln_ >> 4); \
            if (w & 1) { DIAG_MASK(sc[2][0]); sc[3][0] = NEG4; DIAG_MASK(sc[3][1]); } \
            else { DIAG_MASK(sc[0][0]); sc[1][0] = NEG4; DIAG_MASK(sc[1][1]); sc[2][0] = NEG4; sc[2][1] = NEG4; sc[3][0] = NEG4; sc[3][1] = NEG4; } } while (0)
#define FIRST_SM() do { ROWREF(0); ROWREF(1); _Pragma("unroll") for (int kb_ = 0; kb_ < 4; ++kb_) { SUBEXP(kb_, 0); SUBEXP(kb_, 1); } } while (0)
#define RD8(base) do { _Pragma("unroll") for (int n = 0; n < 8; ++n) fr[n] = lds_b128((base) + n * 1024); } while (0)
#define STORE_CTX(q0_, blk_, l0_, l1_) do { \
            const int c16_ = lane_id() & 15; \
            const float s0_ = (1.f - GATE(blk_, w, c16_)) / (l0_), s1_ = (1.f - GATE(blk_, w, 16 + c16_)) / (l1_); \
            const char* sa_ = ctx + (size_t)((8 * (q0_) + 2 * w) * 144 + 16 + 4 * h) * 1024; const char* sb_ = sa_ + 144 * 1024; \
            gstore<0>(sa_, lane * 16, pack44s(o[0][0], o[1][0], s0_));    gstore<1024>(sa_, lane * 16, pack44s(o[2][0], o[3][0], s0_)); \
            gstore<2048>(sa_, lane * 16, pack44s(o[4][0], o[5][0], s0_)); gstore<3072>(sa_, lane * 16, pack44s(o[6][0], o[7][0], s0_)); \
            gstore<0>(sb_, lane * 16, pack44s(o[0][1], o[1][1], s1_));    gstore<1024>(sb_, lane * 16, pack44s(o[2][1], o[3][1], s1_)); \
            gstore<2048>(sb_, lane * 16, pack44s(o[4][1], o[5][1], s1_)); gstore<3072>(sb_, lane * 16, pack44s(o[6][1], o[7][1], s1_)); \
            _Pragma("unroll") for (int db = 0; db < 8; ++db) { o[db][0] = f32x4{}; o[db][1] = f32x4{}; } \
            } while (0)
#define EPILOGUE(q0_, blk_, BAD) do { \
            const float l0 = grp_sum(ls[0]), l1 = grp_sum(ls[1]); \
            f32x4 t_ = o[0][0] + o[0][1]; \
            _Pragma("unroll") for (int db = 1; db < 8; ++db) t_ += o[db][0] + o[db][1]; \
            BAD = !__all(l0 <= L_LIMIT && l1 <= L_LIMIT && finite_f((t_[0] + t_[1]) + (t_[2] + t_[3]))); \
            STORE_CTX(q0_, blk_, l0, l1); \
            ls[0] = 0.f; ls[1] = 0.f; } while (0)
        bool bad0 = false, bad1 = false, early = true;
#define STEP(LAST, NXT) do { \
            const lds_cptr kb_ = LADDR(kc), vb_ = LADDR(vp), nb_ = LADDR(NXT); \
            SBAR(); ALL16(QK_GAP, false, true, kb_, vb_, true, !(LAST)); \
            ARRIVE(8, 8); \
            if (LAST) { MASK_TILE(); SBAR(); } \
            ALL16(PV_GAP, vb_, nb_, (LAST) ? 3 : 1, true); \
            ROT(); } while (0)
#pragma unroll 1
        for (int blk = 0; blk < 2; ++blk) {
            const int q0 = blk ? qs : qf, ntw = 2 * q0 + 1 + (w >> 1);
            if (blk == 1 && w >= 2) {
                const lds_cptr kb_ = LADDR(kc), vb_ = LADDR(vp), nb_ = LADDR(kn);
                SBAR(); ALL16(QK_GAP, true, true, kb_, vb_, true, false);
                ARRIVE(8, 8);
                ALL16(PV_GAP, vb_, nb_, 2, true);
                EPILOGUE(qf, 0, bad0);
            } else {
                const lds_cptr kb_ = LADDR(kc);
                RD8(kb_);
                if (blk) { DMA4(ksrc, kp); DMA4(vsrc, vq); }
                SBAR(); ALL16(QK_GAP, true, false, kb_, kb_, false, false);
                ARRIVE(8, 0);
                if (ntw == 1) MASK_TILE();
                FIRST_SM();
                POLL(0); AWAIT();
                const lds_cptr nb_ = LADDR(ntw == 1 ? vc : kn);
                RD8(nb_);
            }
            ALL8(FIN, 0);
            ROT();
#pragma unroll 1
            for (int t = 1; t + 1 < ntw; ++t) {
                STEP(false, kn);
                if (early && blk == 1 && !bad0) {
                    early = false;
                    if (lane_id() == 0) flag_set(ws, FL_EARLY + 64 * qf + 4 * (h >> 1) + w, tok);
                }
            }
            const bool chain = (blk == 0 && w >= 2);
            if (ntw > 1) STEP(true, chain ? kn : vc);
            if (!chain) {
                const lds_cptr vb_ = LADDR(vp);
                if (w < 2) { DMA4(ksrc, kp); DMA4(vsrc, vq); }
                ALL8(FIN, 1);
                SBAR();
                ALL16(PV_GAP, vb_, vb_, 0, false);
                if (w < 2) ARRIVE(8, 0);
                if (blk == 0) { POLL(4); AWAIT_(4, 4); EPILOGUE(qf, 0, bad0); POLL(0); AWAIT(); ROT(); } else { EPILOGUE(qs, 1, bad1); }
            }
            if (blk == 0) {
                POLL(4); AWAIT_(4, 4);
                const lds_cptr pb_ = (lds_cptr)(uintptr_t)(lds0 + lane_id() * 16 + PARK + w * 8192);
#pragma unroll
                for (int ds = 0; ds < 8; ++ds) q[ds] = lds_b128(pb_ + ds * 1024);
            }
        }
        if (__builtin_expect(bad0 || bad1, 0)) {
            __builtin_amdgcn_fence(__ATOMIC_ACQUIRE, "agent");
#pragma unroll 1
            for (int blk = 1; blk >= 0; --blk) {
                if (blk ? bad1 : bad0) {
                    const int q0 = blk ? qs : qf;
                    const int lane = lane_id();
                    if (blk == 0) q_slow(args()->x + (size_t)(128 * qf + 32 * w) * 1024, args()->wqB + h * 256, lane, q);
                    float l[2];
                    b_slow(kvh, lane, q, o, l, 128 * q0 + 32 * w, 2 * q0 + 1 + (w >> 1));
                    STORE_CTX(q0, blk, grp_sum(l[0]), grp_sum(l[1]));
                }
            }
        }
        STAMP(ws, 6);
        asm volatile("s_waitcnt vmcnt(0)" ::: "memory");
        if (lane_id() == 0) { if (early) flag_set(ws, FL_EARLY + 64 * qf + 4 * (h >> 1) + w, tok); flag_set(ws, FL_LATE + 128 * qs + 4 * (h >> 1) + w, tok); }
    } else {
        const int a = w - 4;
        const char* xsrc = args()->x + (size_t)(128 * qs + 32 * a) * 1024;
        const char* wasrc = args()->wqA + (size_t)(16 * a) * 1024 + (h >> 1) * 64;
        bf16x8 kvo[16];
        kv_proj<1>(lds, b, w, h, xsrc, wqsrc, wasrc, kvo, tok);
        char* const ws = args()->ws;
        const char* kva = ws + WS_KVA;
        const char* ctx = ws + WS_CTX;
        bf16x8 q0, q1;
        int lane;
        {
            f32x4 acc[8][2];
            f32x16 acca;
            q_proj<1>(lds, ws, acc, acca, xsrc, wqsrc, wasrc, w, h, nullptr, 0, i, kvo, tok);
            WAIT_BAR(0);
            lane = lane_id();
            asm volatile("s_mov_b32 m0, %2\n\ts_nop 0\n\tglobal_load_lds_dword %0, %1 sc1" ::"v"(lane * 4), "s"(ws + WS_GATE + (a < 2 ? 128 * qf + 64 * a : 128 * qs + 64 * (a - 2)) * 4),
                         "s"(lds0 + GTAB + a * 256) : "memory");
            __builtin_amdgcn_s_setprio(A_PRIO);
#pragma unroll
            for (int ds = 0; ds < 4; ++ds) {
                lds_st_b128((lds_ptr)lds + PARK + a * 8192 + ds * 1024 + lane * 16, pack44s(acc[2 * ds][0], acc[2 * ds + 1][0], QSCALE_B));
                lds_st_b128((lds_ptr)lds + PARK + a * 8192 + (4 + ds) * 1024 + lane * 16, pack44s(acc[2 * ds][1], acc[2 * ds + 1][1], QSCALE_B));
            }
            asm volatile("s_waitcnt vmcnt(0) lgkmcnt(0)\n\ts_mov_b64 exec, 1\n\tds_add_u32 %0, %1\n\ts_mov_b64 exec, -1" ::"v"(lds0 + CNT + 4), "v"(1u) : "memory");
            q0 = pack8s<0>(acca, QSCALE_A); q1 = pack8s<1>(acca, QSCALE_A);
        }
        const __amdgpu_buffer_rsrc_t kva_rs = __builtin_amdgcn_make_buffer_rsrc((void*)kva, 0, 32 * 6144, 0x00020000);
#define LDF(off) __builtin_bit_cast(bf16x8, __builtin_amdgcn_raw_buffer_load_b128(kva_rs, lane * 16, (off), 16))
#define LDK(K0, K1, t) do { K0 = LDF((t) * 6144); K1 = LDF((t) * 6144 + 1024); } while (0)
#define LDV(V, t) do { _Pragma("unroll") for (int u = 0; u < 4; ++u) V[u] = LDF((t) * 6144 + 2048 + u * 1024); } while (0)
        bf16x8 kk[4][2], vv[4][4];
        LDK(kk[0][0], kk[0][1], 0); LDV(vv[0], 0); LDK(kk[1][0], kk[1][1], 1); LDV(vv[1], 1); LDK(kk[2][0], kk[2][1], 2); LDV(vv[2], 2); LDK(kk[3][0], kk[3][1], 3);
        if (WARM2) {
            const int n = b * 4 + a;
            const char *p0 = args()->woA, *p1 = args()->woB;
            asm volatile("" : "+s"(p0), "+s"(p1));
            if (n < 576) touch(uptr(n < 64 ? p0 + (size_t)n * 8192 : p1 + (size_t)(n - 64) * 8192), lane * 128, lds0 + SCR);
        }
        STAMP(ws, 4);
        f32x16 o0 = f32x16{}, o1 = f32x16{}, n0, n1v, s00, s01, s10, s11;
        {
            s00 = MFMA(kk[0][0], q0, f32x16{}); s01 = MFMA(kk[0][1], q0, f32x16{}); s10 = MFMA(kk[0][0], q1, f32x16{}); s11 = MFMA(kk[0][1], q1, f32x16{});
            const float m0 = rowmax32(s00, s01), m1 = rowmax32(s10, s11);
#pragma unroll
            for (int e = 0; e < 16; ++e) { n0[e] = -m0; s00[e] -= m0; s01[e] -= m0; n1v[e] = -m1; s10[e] -= m1; s11[e] -= m1; }
            PIN(n0); PIN(n1v);
        }
#define A_GRP(S, hf, V, O) do { _Pragma("unroll") for (int e = 0; e < 8; ++e) S[8 * (hf) + e] = ex2(S[8 * (hf) + e]); \
            { const bf16x8 pk_ = pack8<hf>(S); SBAR(); APRIO_HI(); O = MFMA(V, pk_, O); PIN(O); APRIO_LO(); } SBAR(); } while (0)
#define A_TILE(t, u) do { \
            { const int tk_ = (t) + 4 < 32 ? (t) + 4 : 31, tv_ = (t) + 3 < 32 ? (t) + 3 : 31; LDK(kk[u][0], kk[u][1], tk_); LDV(vv[((u) + 3) & 3], tv_); } \
            SBAR(); \
            A_GRP(s00, 0, vv[u][0], o0); A_GRP(s00, 1, vv[u][1], o0); A_GRP(s01, 0, vv[u][2], o0); A_GRP(s01, 1, vv[u][3], o0); \
            APRIO_HI(); s00 = MFMA(kk[((u) + 1) & 3][0], q0, n0); s01 = MFMA(kk[((u) + 1) & 3][1], q0, n0); PIN(s00); PIN(s01); APRIO_LO(); \
            SBAR(); \
            A_GRP(s10, 0, vv[u][0], o1); A_GRP(s10, 1, vv[u][1], o1); A_GRP(s11, 0, vv[u][2], o1); A_GRP(s11, 1, vv[u][3], o1); \
            APRIO_HI(); s10 = MFMA(kk[((u) + 1) & 3][0], q1, n1v); s11 = MFMA(kk[((u) + 1) & 3][1], q1, n1v); PIN(s10); PIN(s11); APRIO_LO(); \
            SBAR(); } while (0)
#pragma unroll 1
        for (int t = 0; t < 32; t += 4) { A_TILE(t, 0); A_TILE(t + 1, 1); A_TILE(t + 2, 2); A_TILE(t + 3, 3); }
        {
            bool ok = o0[8] <= L_LIMIT && o1[8] <= L_LIMIT;
#pragma unroll
            for (int e = 0; e < 8; ++e) ok = ok && finite_f(o0[e]) && finite_f(o1[e]);
            if (__builtin_expect(!__all(ok), 0)) {
                __builtin_amdgcn_fence(__ATOMIC_ACQUIRE, "agent");
                a_slow(kva, lane, q0, o0); a_slow(kva, lane, q1, o1);
            }
            const float g_s = GATE(1, a, lane & 31);
            const u32x4 p0 = __builtin_bit_cast(u32x4, pack8s<0>(o0, g_s / o0[8])), p1 = __builtin_bit_cast(u32x4, pack8s<0>(o1, g_s / o1[8]));
            const lds_ptr tp = (lds_ptr)lds + (a < 2 ? 49152 : 98304) + a * 2048;
            const lds_ptr wp = tp + ((lane >> 4) & 1) * 1024 + ((lane & 15) + 16 * (lane >> 5)) * 16;
            *(__attribute__((address_space(3))) u32x4*)wp = u32x4{p0[0], p0[1], p1[0], p1[1]};
            *(__attribute__((address_space(3))) u32x4*)(wp + 512) = u32x4{p0[2], p0[3], p1[2], p1[3]};
            asm volatile("s_waitcnt lgkmcnt(0)" ::: "memory");
            const char* dst = ctx + ((size_t)(8 * qs + 2 * a) * 144 + (h >> 1)) * 1024;
            gstore<0>(dst, lane * 16, lds_b128(tp + lane * 16));
            gstore<0>(dst + 144 * 1024, lane * 16, lds_b128(tp + 1024 + lane * 16));
        }
        STAMP(ws, 6);
        asm volatile("s_waitcnt vmcnt(0)" ::: "memory");
        if (lane_id() == 0) flag_set(ws, FL_LATE + 128 * qs + 64 + 4 * (h >> 1) + a, tok);
        if (WARM3) {
            const char *p0 = args()->woA, *p1 = args()->woB;
            asm volatile("" : "+s"(p0), "+s"(p1));
#pragma unroll
            for (int u = 0; u < 3; ++u) {
                const int n = (j * 4 + a) * 3 + u;
                if (n < 288) touch(uptr((n < 32 ? p0 + (size_t)n * 16384 : p1 + (size_t)(n - 32) * 16384) + (b & 1) * 512), (lane >> 2) * 1024 + (lane & 3) * 128, lds0 + SCR);
            }
            asm volatile("s_waitcnt vmcnt(0)" ::: "memory");
        }
    }
    out_proj(lds, b, w, tok);
    asm volatile("gm_code_end:" ::: "memory");
}

extern "C" void kernel_launch(void* const* d_in, const int* in_sizes, int n_in, void* d_out, int out_size, void* d_ws, size_t ws_size,
                              hipStream_t stream) {
    (void)in_sizes; (void)n_in; (void)out_size; (void)ws_size;
    Args A;
    A.x = (const char*)d_in[0]; A.wqA = (const char*)d_in[1]; A.wk = (const char*)d_in[2]; A.wv = (const char*)d_in[3]; A.woA = (const char*)d_in[4];
    A.ckv = (const char*)d_in[6]; A.wqB = (const char*)d_in[7]; A.wuk = (const char*)d_in[8]; A.wuv = (const char*)d_in[9]; A.woB = (const char*)d_in[10];
    A.gate_w = (const char*)d_in[11]; A.ws = (char*)d_ws; A.out = (char*)d_out;
    gated_mix_fused<<<256, 512, 0, stream>>>(A);
}
```
